```python
import torch

def _softmax(x, axis=-1):
    return torch.softmax(x, dim=axis)

def _gelu(x):
    return torch.nn.functional.gelu(x, approximate='tanh')

def _silu(x):
    return torch.nn.functional.silu(x)

def _sigmoid(x):
    return torch.sigmoid(x)

def _layernorm(x, axis=-1, eps=1e-05):
    m = x.mean(dim=axis, keepdim=True)
    v = x.var(dim=axis, keepdim=True, unbiased=False)
    return (x - m) / torch.sqrt(v + eps)

def _scatter_add(tgt, idx, src, axis=0):
    return tgt.clone().index_add_(axis, idx, src)

def _segment_sum(src, seg, n):
    out = torch.zeros((n,) + src.shape[1:], dtype=src.dtype)
    return out.index_add_(0, seg, src)

def _pad(x, pw):
    flat = tuple((v for pair in reversed(pw) for v in pair))
    return torch.nn.functional.pad(x, flat)
kernel_name = 'adaLN_cond__gqa_block_cached__swiglu_ffn__decode'

def setup_inputs():

    _w = lambda a: (lambda t: t.to(torch.bfloat16) if t.is_floating_point() else t)(torch.as_tensor(a))

    def _A_setup():
        B, Sq, D = (2, 1, 448)
        Hq, Hkv, Sk, Dh = (16, 8, 8192, 256)
        assert Hq % Hkv == 0, (Hq, Hkv)
        return {'x': _w((lambda *s: torch.randn(*s, dtype=torch.bfloat16) * 0.05)(B, Sq, D)), 'k_cache': _w((lambda *s: torch.randn(*s, dtype=torch.bfloat16) * 3.2)(B, Hkv, Sk, Dh)), 'v_cache': _w((lambda *s: torch.randn(*s, dtype=torch.bfloat16) * 0.4)(B, Hkv, Sk, Dh)), 'wq': _w((lambda *s: torch.randn(*s, dtype=torch.bfloat16) * 3.2)(D, Hq * Dh) * D ** (-0.5)), 'wo': _w((lambda *s: torch.randn(*s, dtype=torch.bfloat16) * 0.4)(Hq * Dh, D) * (Hq * Dh) ** (-0.5))}
    _A_in = _A_setup()

    def _B_setup():
        B, S, D, F = (2, 1, 448, 1792)
        return {'x': _w((lambda *s: torch.randn(*s, dtype=torch.bfloat16) * 0.05)(B, S, D)), 'w_gate': _w((lambda *s: torch.randn(*s, dtype=torch.bfloat16) * 0.05)(D, F)), 'w_up': _w((lambda *s: torch.randn(*s, dtype=torch.bfloat16) * 0.05)(D, F)), 'w_down': _w((lambda *s: torch.randn(*s, dtype=torch.bfloat16) * 0.05)(F, D))}
    _B_in = _B_setup()
    return {'A_x': _A_in['x'], 'A_k_cache': _A_in['k_cache'], 'A_v_cache': _A_in['v_cache'], 'A_wq': _A_in['wq'], 'A_wo': _A_in['wo'], 'B_x': _B_in['x'], 'B_w_gate': _B_in['w_gate'], 'B_w_up': _B_in['w_up'], 'B_w_down': _B_in['w_down'], 'c': _w((lambda *s: torch.randn(*s, dtype=torch.bfloat16) * 0.2)(2, 448) * 8.0), 'w_mod': _w((lambda *s: torch.randn(*s, dtype=torch.bfloat16) * 0.2)(448, 6 * 448) * 448 ** (-0.5) * 8.0)}

def reference(A_x, A_k_cache, A_v_cache, A_wq, A_wo, B_x, B_w_gate, B_w_up, B_w_down, c, w_mod):

    def _A_ref(x, k_cache, v_cache, wq, wo):
        B, Sq, D = x.shape
        Hq, Dh = (16, 256)
        Hkv = k_cache.shape[1]
        rep = Hq // Hkv
        q = (lambda x, *d: x.permute(*d))((x @ wq).reshape(B, Sq, Hq, Dh), 0, 2, 1, 3)
        k = (lambda x, n, axis: torch.repeat_interleave(x, n, dim=axis))(k_cache, rep, axis=1)
        v = (lambda x, n, axis: torch.repeat_interleave(x, n, dim=axis))(v_cache, rep, axis=1)
        scores = torch.einsum('bhqd,bhkd->bhqk', q, k) * Dh ** (-0.5)
        p = _softmax(scores, axis=-1)
        o = torch.einsum('bhqk,bhkd->bhqd', p, v)
        return (lambda x, *d: x.permute(*d))(o, 0, 2, 1, 3).reshape(B, Sq, Hq * Dh) @ wo

    def _B_ref(x, w_gate, w_up, w_down):
        g = _silu(x @ w_gate)
        u = x @ w_up
        return g * u @ w_down
    x = A_x
    mod = (_silu(c) @ w_mod).reshape(c.shape[0], 6, 1, -1)
    sa, ca, ga, sb, cb, gb = (mod[:, 0].clone(), mod[:, 1].clone(), mod[:, 2].clone(), mod[:, 3].clone(), mod[:, 4].clone(), mod[:, 5].clone())
    h = _layernorm(x, axis=-1) * (1 + ca) + sa
    x = x + ga * _A_ref(h, A_k_cache, A_v_cache, A_wq, A_wo)
    h = _layernorm(x, axis=-1) * (1 + cb) + sb
    x = x + gb * _B_ref(h, B_w_gate, B_w_up, B_w_down)
    return x
```

```cpp
#include <hip/hip_runtime.h>
#include <hip/hip_bf16.h>
#include <stdint.h>

typedef unsigned short u16;
typedef unsigned u32x4 __attribute__((ext_vector_type(4)));
typedef __bf16 bf16x2 __attribute__((ext_vector_type(2)));

constexpr int D = 448, HQ = 16, HKV = 8, SK = 8192, DH = 256, F = 1792, NQ = HQ * DH;

__device__ __forceinline__ float bf2f(u16 b) { return __uint_as_float(((unsigned)b) << 16); }
__device__ __forceinline__ float rbf(float f) { return (float)(__bf16)f; }
__device__ __forceinline__ u16 f2bf(float f) { __bf16 b = (__bf16)f; return __builtin_bit_cast(u16, b); }
__device__ __forceinline__ float dot2(unsigned a, unsigned b, float c) {
  return __builtin_amdgcn_fdot2_f32_bf16(__builtin_bit_cast(bf16x2, a), __builtin_bit_cast(bf16x2, b), c, false);
}
__device__ __forceinline__ float wave_sum(float v) {
  for (int o = 32; o > 0; o >>= 1) v += __shfl_xor(v, o);
  return v;
}
__device__ __forceinline__ float wave_max(float v) {
  for (int o = 32; o > 0; o >>= 1) v = fmaxf(v, __shfl_xor(v, o));
  return v;
}

template <int K, int N>
__device__ __forceinline__ void gemv64(const float* sin  , const u16* W, int n0, float* red  , float& o0, float& o1) {
  const int lane = threadIdx.x & 63, wave = threadIdx.x >> 6;
  float a0 = 0.f, a1 = 0.f;
  for (int k = wave; k < K; k += 4) {
    float w = bf2f(W[(size_t)k * N + n0 + lane]);
    a0 = fmaf(sin[k], w, a0);
    a1 = fmaf(sin[K + k], w, a1);
  }
  red[(wave * 2 + 0) * 64 + lane] = a0;
  red[(wave * 2 + 1) * 64 + lane] = a1;
  __syncthreads();
  o0 = red[0 * 64 + lane] + red[2 * 64 + lane] + red[4 * 64 + lane] + red[6 * 64 + lane];
  o1 = red[1 * 64 + lane] + red[3 * 64 + lane] + red[5 * 64 + lane] + red[7 * 64 + lane];
  __syncthreads();
}

__global__ void __launch_bounds__(256) k_mod(const u16* c, const u16* wmod, float* mod) {
  __shared__ float s[2 * D];
  __shared__ float red[8 * 64];
  for (int i = threadIdx.x; i < 2 * D; i += 256) { float x = bf2f(c[i]); s[i] = rbf(x / (1.f + __expf(-x))); }
  __syncthreads();
  float o0, o1;
  gemv64<D, 6 * D>(s, wmod, blockIdx.x * 64, red, o0, o1);
  if (threadIdx.x < 64) { mod[blockIdx.x * 64 + threadIdx.x] = rbf(o0); mod[6 * D + blockIdx.x * 64 + threadIdx.x] = rbf(o1); }
}

__device__ __forceinline__ void adaln(const float* sx, const float* mod, int ish, int isc, float* sh) {
  const int lane = threadIdx.x & 63, wave = threadIdx.x >> 6;
  if (wave < 2) {
    const float* x = sx + wave * D;
    float s = 0.f;
    for (int i = lane; i < D; i += 64) s += x[i];
    s = wave_sum(s);
    float mean = s * (1.f / D);
    float v = 0.f;
    for (int i = lane; i < D; i += 64) { float d = x[i] - mean; v += d * d; }
    v = wave_sum(v) * (1.f / D);
    float mb = rbf(mean), vb = rbf(v);
    float den = rbf(sqrtf(rbf(vb + 1e-5f)));
    for (int i = lane; i < D; i += 64) {
      float ln = rbf(rbf(x[i] - mb) / den);
      float sc = rbf(1.f + mod[wave * 6 * D + isc * D + i]);
      sh[wave * D + i] = rbf(rbf(ln * sc) + mod[wave * 6 * D + ish * D + i]);
    }
  }
  __syncthreads();
}

__global__ void __launch_bounds__(256) k_q(const u16* x, const float* mod, const u16* wq, u16* q) {
  __shared__ float sx[2 * D], sh[2 * D];
  __shared__ float red[8 * 64];
  for (int i = threadIdx.x; i < 2 * D; i += 256) sx[i] = bf2f(x[i]);
  __syncthreads();
  adaln(sx, mod, 0, 1, sh);
  float o0, o1;
  gemv64<D, NQ>(sh, wq, blockIdx.x * 64, red, o0, o1);
  if (threadIdx.x < 64) { q[blockIdx.x * 64 + threadIdx.x] = f2bf(o0); q[NQ + blockIdx.x * 64 + threadIdx.x] = f2bf(o1); }
}

constexpr int NSPLIT = 16, KPW = SK / NSPLIT;
__global__ void __launch_bounds__(512) k_attn(const u16* q, const u16* kc, const u16* vc, float* part_o, float* part_ml) {
  __shared__ float sp[2][KPW];
  __shared__ float so[8][2 * DH];
  __shared__ float sm[8][2];
  const int bid = blockIdx.x, grp = bid / NSPLIT, sp_i = bid % NSPLIT, b = grp / HKV, kvh = grp % HKV;
  const int lane = threadIdx.x & 63, wave = threadIdx.x >> 6;
  const size_t base = ((size_t)grp * SK + sp_i * KPW + wave * 64) * DH;
  const u32x4* kp = (const u32x4*)(kc + base) + lane;
  const u32x4* vp = (const u32x4*)(vc + base) + lane;
  u32x4 kr[32];
#pragma unroll
  for (int i = 0; i < 32; ++i) kr[i] = __builtin_nontemporal_load(kp + i * 64);
  u32x4 q0 = *(const u32x4*)(q + b * NQ + (kvh * 2 + 0) * DH + (lane & 31) * 8);
  u32x4 q1 = *(const u32x4*)(q + b * NQ + (kvh * 2 + 1) * DH + (lane & 31) * 8);
#pragma unroll
  for (int i = 0; i < 32; ++i) {
    float a0 = 0.f, a1 = 0.f;
#pragma unroll
    for (int j = 0; j < 4; ++j) { a0 = dot2(kr[i][j], q0[j], a0); a1 = dot2(kr[i][j], q1[j], a1); }
    for (int o = 16; o > 0; o >>= 1) { a0 += __shfl_xor(a0, o); a1 += __shfl_xor(a1, o); }
    if ((lane & 31) == 0) { int key = wave * 64 + i * 2 + (lane >> 5); sp[0][key] = rbf(a0) * 0.0625f; sp[1][key] = rbf(a1) * 0.0625f; }
  }
  u32x4 vr[32];
#pragma unroll
  for (int i = 0; i < 32; ++i) vr[i] = __builtin_nontemporal_load(vp + i * 64);
  __syncthreads();
  float s0 = sp[0][threadIdx.x], s1 = sp[1][threadIdx.x];
  float m0 = wave_max(s0), m1 = wave_max(s1);
  if (lane == 0) { sm[wave][0] = m0; sm[wave][1] = m1; }
  __syncthreads();
  for (int w = 0; w < 8; ++w) { m0 = fmaxf(m0, sm[w][0]); m1 = fmaxf(m1, sm[w][1]); }
  float p0 = __expf(s0 - m0), p1 = __expf(s1 - m1);
  sp[0][threadIdx.x] = p0; sp[1][threadIdx.x] = p1;
  float l0 = wave_sum(p0), l1 = wave_sum(p1);
  __syncthreads();
  if (lane == 0) { sm[wave][0] = l0; sm[wave][1] = l1; }
  float acc[16];
#pragma unroll
  for (int j = 0; j < 16; ++j) acc[j] = 0.f;
#pragma unroll
  for (int i = 0; i < 32; ++i) {
    int key = wave * 64 + i * 2 + (lane >> 5);
    float pp0 = sp[0][key], pp1 = sp[1][key];
#pragma unroll
    for (int j = 0; j < 4; ++j) {
      float lo = __uint_as_float(vr[i][j] << 16), hi = __uint_as_float(vr[i][j] & 0xffff0000u);
      acc[2 * j] = fmaf(pp0, lo, acc[2 * j]); acc[2 * j + 1] = fmaf(pp0, hi, acc[2 * j + 1]);
      acc[8 + 2 * j] = fmaf(pp1, lo, acc[8 + 2 * j]); acc[8 + 2 * j + 1] = fmaf(pp1, hi, acc[8 + 2 * j + 1]);
    }
  }
#pragma unroll
  for (int j = 0; j < 16; ++j) acc[j] += __shfl_xor(acc[j], 32);
  if (lane < 32) {
#pragma unroll
    for (int j = 0; j < 8; ++j) { so[wave][lane * 8 + j] = acc[j]; so[wave][DH + lane * 8 + j] = acc[8 + j]; }
  }
  __syncthreads();
  float t = 0.f;
  for (int w = 0; w < 8; ++w) t += so[w][threadIdx.x];
  part_o[(size_t)bid * 512 + threadIdx.x] = t;
  if (threadIdx.x < 2) {
    float l = 0.f;
    for (int w = 0; w < 8; ++w) l += sm[w][threadIdx.x];
    part_ml[bid * 4 + threadIdx.x * 2] = threadIdx.x ? m1 : m0;
    part_ml[bid * 4 + threadIdx.x * 2 + 1] = l;
  }
}

__global__ void __launch_bounds__(512) k_comb(const float* part_o, const float* part_ml, float* o) {
  const int grp = blockIdx.x, t = threadIdx.x, h = t >> 8;
  float M = -INFINITY;
  for (int s = 0; s < NSPLIT; ++s) M = fmaxf(M, part_ml[(grp * NSPLIT + s) * 4 + h * 2]);
  float L = 0.f, a = 0.f;
  for (int s = 0; s < NSPLIT; ++s) {
    float w = __expf(part_ml[(grp * NSPLIT + s) * 4 + h * 2] - M);
    L += w * part_ml[(grp * NSPLIT + s) * 4 + h * 2 + 1];
    a += w * part_o[(size_t)(grp * NSPLIT + s) * 512 + t];
  }
  o[grp * 512 + t] = rbf(a / L);
}

__global__ void __launch_bounds__(256) k_wo(const float* o, const u16* wo, const u16* x, const float* mod, float* x1) {
  __shared__ float s[2 * NQ];
  __shared__ float red[8 * 64];
  for (int i = threadIdx.x; i < 2 * NQ; i += 256) s[i] = o[i];
  __syncthreads();
  float o0, o1;
  gemv64<NQ, D>(s, wo, blockIdx.x * 64, red, o0, o1);
  if (threadIdx.x < 64) {
    int n = blockIdx.x * 64 + threadIdx.x;
    x1[n] = rbf(bf2f(x[n]) + rbf(mod[2 * D + n] * rbf(o0)));
    x1[D + n] = rbf(bf2f(x[D + n]) + rbf(mod[6 * D + 2 * D + n] * rbf(o1)));
  }
}

__global__ void __launch_bounds__(256) k_gu(const float* x1, const float* mod, const u16* wg, const u16* wu, float* act) {
  __shared__ float sx[2 * D], sh[2 * D];
  __shared__ float red[8 * 64];
  for (int i = threadIdx.x; i < 2 * D; i += 256) sx[i] = x1[i];
  __syncthreads();
  adaln(sx, mod, 3, 4, sh);
  float g0, g1, u0, u1;
  gemv64<D, F>(sh, wg, blockIdx.x * 64, red, g0, g1);
  gemv64<D, F>(sh, wu, blockIdx.x * 64, red, u0, u1);
  if (threadIdx.x < 64) {
    int n = blockIdx.x * 64 + threadIdx.x;
    g0 = rbf(g0); g1 = rbf(g1);
    g0 = rbf(g0 / (1.f + __expf(-g0))); g1 = rbf(g1 / (1.f + __expf(-g1)));
    act[n] = rbf(g0 * rbf(u0)); act[F + n] = rbf(g1 * rbf(u1));
  }
}

__global__ void __launch_bounds__(256) k_down(const float* act, const u16* wd, const float* x1, const float* mod, u16* out) {
  __shared__ float s[2 * F];
  __shared__ float red[8 * 64];
  for (int i = threadIdx.x; i < 2 * F; i += 256) s[i] = act[i];
  __syncthreads();
  float o0, o1;
  gemv64<F, D>(s, wd, blockIdx.x * 64, red, o0, o1);
  if (threadIdx.x < 64) {
    int n = blockIdx.x * 64 + threadIdx.x;
    out[n] = f2bf(x1[n] + rbf(mod[5 * D + n] * rbf(o0)));
    out[D + n] = f2bf(x1[D + n] + rbf(mod[6 * D + 5 * D + n] * rbf(o1)));
  }
}

extern "C" void kernel_launch(void* const* d_in, const int* in_sizes, int n_in, void* d_out, int out_size, void* d_ws, size_t ws_size,
                              hipStream_t stream) {
  const u16* x = (const u16*)d_in[0];
  const u16* kc = (const u16*)d_in[1];
  const u16* vc = (const u16*)d_in[2];
  const u16* wq = (const u16*)d_in[3];
  const u16* wo = (const u16*)d_in[4];
  const u16* wg = (const u16*)d_in[6];
  const u16* wu = (const u16*)d_in[7];
  const u16* wd = (const u16*)d_in[8];
  const u16* c = (const u16*)d_in[9];
  const u16* wmod = (const u16*)d_in[10];
  char* ws = (char*)d_ws;
  float* mod = (float*)(ws);
  u16* q = (u16*)(ws + 32768);
  float* part_ml = (float*)(ws + 65536);
  float* o = (float*)(ws + 81920);
  float* x1 = (float*)(ws + 131072);
  float* act = (float*)(ws + 139264);
  float* part_o = (float*)(ws + 262144);
  k_mod<<<6 * D / 64, 256, 0, stream>>>(c, wmod, mod);
  k_q<<<NQ / 64, 256, 0, stream>>>(x, mod, wq, q);
  k_attn<<<256, 512, 0, stream>>>(q, kc, vc, part_o, part_ml);
  k_comb<<<16, 512, 0, stream>>>(part_o, part_ml, o);
  k_wo<<<D / 64, 256, 0, stream>>>(o, wo, x, mod, x1);
  k_gu<<<F / 64, 256, 0, stream>>>(x1, mod, wg, wu, act);
  k_down<<<D / 64, 256, 0, stream>>>(act, wd, x1, mod, (u16*)d_out);
}
```

```cpp
#include <hip/hip_runtime.h>
#include <stdint.h>

typedef unsigned short u16;
typedef unsigned u32;
typedef u32 u32x2 __attribute__((ext_vector_type(2)));
typedef u32 u32x4 __attribute__((ext_vector_type(4)));
typedef float f32x2 __attribute__((ext_vector_type(2)));
typedef float f32x4 __attribute__((ext_vector_type(4)));
typedef short s16x4 __attribute__((ext_vector_type(4)));
typedef __bf16 bf16x2 __attribute__((ext_vector_type(2)));
typedef __bf16 bf16x8 __attribute__((ext_vector_type(8)));
typedef __amdgpu_buffer_rsrc_t rsrc_t;
#define LAS __attribute__((address_space(3)))
#define DEVI __device__ __forceinline__

constexpr int D = 448, NQ = 4096, FF = 1792, NMOD = 6 * D;
constexpr int AUX_SC1 = 16, AUX_STREAM = 18;
constexpr u32 SPIN_CAP = 1u << 17, LSPIN_CAP = 1u << 23;
constexpr int TAU = 281, TAU_END = 301, N_END = 46;
constexpr int TOFF = 500, TAUMIN = 235;
constexpr int LEAD_FFN = 850, LEAD_NONE = 350;
constexpr int PACE = 6;
constexpr int MOD_AT = 16, Q_AT = 30, VPRE = 8, MD_AT = 19, PV0_AT = 21, STAT_AT = 25;
constexpr int H1_DELAY = 1700, H2_DELAY = 2820, H4_DELAY = 800;
constexpr int CODE_BYTES = 24896;
constexpr int WS_EPOCH = 0, WS_MOD = 4096, WS_TS = 32768, WS_Q = 65536, WS_ST = 131072, WS_AP = 163840, WS_DP = 262144, WS_OP = 524288, WS_END = 1 << 20;
constexpr int L_RED = 0;
constexpr int L_Q = L_RED + 16384;
constexpr int L_P = L_Q + 1024;
constexpr int L_ACT = L_P + 2048;
constexpr int L_PD = L_ACT + 8192;
constexpr int L_A2 = L_PD + 16384;
constexpr int L_X1 = L_A2 + 512;
constexpr int L_SS = L_X1 + 1792;
constexpr int L_CNT = L_SS + 128;
constexpr int L_F = L_CNT + 64;
constexpr int L_Z = L_CNT + 128;
constexpr int L_WD = L_Z + 512;
constexpr int L_W = L_WD + 28672;
constexpr int L_END = L_W + 65536;
static_assert(L_WD % 16 == 0 && L_END <= 160 * 1024 && L_END > 80 * 1024, "LDS map");

DEVI float bf_lo(u32 v) { return __uint_as_float(v << 16); }
DEVI float bf_hi(u32 v) { return __uint_as_float(v & 0xffff0000u); }
DEVI u32 pk_bf(float lo, float hi) { bf16x2 r = {(__bf16)lo, (__bf16)hi}; return __builtin_bit_cast(u32, r); }
DEVI float rbf(float f) { return (float)(__bf16)f; }
DEVI float dot2(u32 a, u32 b, float c) { return __builtin_amdgcn_fdot2_f32_bf16(__builtin_bit_cast(bf16x2, a), __builtin_bit_cast(bf16x2, b), c, false); }
DEVI rsrc_t mk_rsrc(const void* p, u32 bytes) { return __builtin_amdgcn_make_buffer_rsrc((void*)p, 0, bytes, 0x00020000); }
template <int AUX> DEVI u32x4 ld128(rsrc_t r, int voff, int soff) { return __builtin_amdgcn_raw_buffer_load_b128(r, voff, soff, AUX); }
template <int AUX> DEVI u32x2 ld64(rsrc_t r, int voff, int soff) { return __builtin_amdgcn_raw_buffer_load_b64(r, voff, soff, AUX); }
template <int AUX> DEVI u32 ld32(rsrc_t r, int voff, int soff) { return __builtin_amdgcn_raw_buffer_load_b32(r, voff, soff, AUX); }
DEVI void st128_sc1(rsrc_t r, u32x4 v, int voff, int soff) { __builtin_amdgcn_raw_buffer_store_b128(v, r, voff, soff, AUX_SC1); }
template <int CTRL> DEVI float dpp(float x) { return __builtin_bit_cast(float, __builtin_amdgcn_mov_dpp(__builtin_bit_cast(int, x), CTRL, 0xf, 0xf, true)); }
constexpr int XOR1 = 0xB1, XOR2 = 0x4E, XOR7 = 0x141, ROR8 = 0x128, ROR4 = 0x124;
DEVI float red32(float a, float b) { auto s = __builtin_amdgcn_permlane32_swap(__float_as_uint(a), __float_as_uint(b), false, false); return __uint_as_float(s[0]) + __uint_as_float(s[1]); }
DEVI float red16(float a, float b) { auto s = __builtin_amdgcn_permlane16_swap(__float_as_uint(a), __float_as_uint(b), false, false); return __uint_as_float(s[0]) + __uint_as_float(s[1]); }
DEVI float max32(float a) { auto s = __builtin_amdgcn_permlane32_swap(__float_as_uint(a), __float_as_uint(a), false, false); return fmaxf(__uint_as_float(s[0]), __uint_as_float(s[1])); }
DEVI float max16(float a) { auto s = __builtin_amdgcn_permlane16_swap(__float_as_uint(a), __float_as_uint(a), false, false); return fmaxf(__uint_as_float(s[0]), __uint_as_float(s[1])); }
DEVI float row_sum(float v) { v += dpp<XOR1>(v); v += dpp<XOR2>(v); v += dpp<XOR7>(v); v += dpp<ROR8>(v); return v; }
DEVI float row_max(float v) { v = fmaxf(v, dpp<XOR1>(v)); v = fmaxf(v, dpp<XOR2>(v)); v = fmaxf(v, dpp<XOR7>(v)); v = fmaxf(v, dpp<ROR8>(v)); return v; }
DEVI float wave_sum(float v) { v = row_sum(v); v = red16(v, v); return red32(v, v); }
DEVI float wave_max(float v) { return max32(max16(row_max(v))); }
DEVI float ex2(float x) { return __builtin_amdgcn_exp2f(x); }
DEVI float rcp(float x) { return __builtin_amdgcn_rcpf(x); }
constexpr float LOG2E = 1.4426950408889634f;
DEVI float silu(float x) { return x * rcp(1.f + ex2(-x * LOG2E)); }
DEVI u32 tagw(float v, u32 tag) { return __builtin_amdgcn_perm(__float_as_uint(v), tag, 0x07060500u); }
DEVI u32x4 tag4(f32x4 v, u32 tag) { return u32x4{tagw(v[0], tag), tagw(v[1], tag), tagw(v[2], tag), tagw(v[3], tag)}; }
DEVI f32x4 as_f4(u32x4 v) { return __builtin_bit_cast(f32x4, v); }
DEVI f32x4 mfma(u32x4 a, u32x4 b, f32x4 c) { return __builtin_amdgcn_mfma_f32_16x16x32_bf16(__builtin_bit_cast(bf16x8, a), __builtin_bit_cast(bf16x8, b), c, 0, 0, 0); }
#define BAR() asm volatile("s_waitcnt lgkmcnt(0)\n\ts_barrier" ::: "memory")
#define SB() __builtin_amdgcn_sched_barrier(0)

#define MEMBAR() asm volatile("" ::: "memory")
#define PIN(x) asm volatile("" : "+v"(x))
#define PINS(x) asm volatile("" : "+s"(x))

template <typename T> DEVI T lds_ld(LAS unsigned char* L, int off) { typedef T __attribute__((may_alias)) A; return *(LAS A*)(L + off); }
template <typename T> DEVI void lds_st(LAS unsigned char* L, int off, T v) { typedef T __attribute__((may_alias)) A; *(LAS A*)(L + off) = v; }
DEVI u32x4 tr_frag(LAS unsigned char* L, int off, int rb4) {
  const s16x4 a = __builtin_amdgcn_ds_read_tr16_b64_v4i16((LAS s16x4*)(L + off));
  const s16x4 b = __builtin_amdgcn_ds_read_tr16_b64_v4i16((LAS s16x4*)(L + off + rb4));
  return __builtin_bit_cast(u32x4, __builtin_shufflevector(a, b, 0, 1, 2, 3, 4, 5, 6, 7));
}
DEVI u32 or4(u32x4 v) { return v[0] | v[1] | v[2] | v[3]; }
DEVI bool bad4(u32x4 v) { return __any((or4(v) & 0xffu) != 0u); }
template <int N, typename OffF> DEVI void gather(rsrc_t ws, int voff, OffF soff, u32 tag, u32x4 (&v)[N]) {
  for (u32 spin = 0; spin < SPIN_CAP; ++spin) {
    MEMBAR();
    u32 bad = 0;
#pragma unroll
    for (int k = 0; k < N; ++k) v[k] = ld128<AUX_SC1>(ws, voff, soff(k));
#pragma unroll
    for (int k = 0; k < N; ++k) { v[k] ^= tag; bad |= or4(v[k]); }
    if (!__any((bad & 0xffu) != 0u)) break;
  }
}
#define CLD(k, o) "s_bitcmp1_b32 %[m], " #k "\n\ts_cbranch_scc0 1f\n\tbuffer_load_dwordx4 %" #k ", %[vo], %[rs], %[" #o "] offen sc1\n1:\n\t"
#define TAKE(v) do { v ^= tag; bad |= or4(v); PIN(bad); } while (0)
#define RECHECK(v, k) if (m & (1u << (k))) { v ^= tag; if (!bad4(v)) m &= ~(1u << (k)); }
DEVI void gather4_issue(rsrc_t ws, int vo, int o0, int o1, int o2, int o3, u32x4& a, u32x4& b, u32x4& c, u32x4& d) {
  MEMBAR();
  a = ld128<AUX_SC1>(ws, vo, o0); b = ld128<AUX_SC1>(ws, vo, o1); c = ld128<AUX_SC1>(ws, vo, o2); d = ld128<AUX_SC1>(ws, vo, o3);
  MEMBAR();
}
DEVI void gather4_finish(u32x4 rs, int vo, int o0, int o1, int o2, int o3, u32 tag, u32x4& a, u32x4& b, u32x4& c, u32x4& d, f32x4& s0, f32x4& s1) {
  u32 bad = 0;
  TAKE(a); SB(); TAKE(b); s0 = as_f4(a) + as_f4(b); PIN(s0); SB(); TAKE(c); SB(); TAKE(d); s1 = as_f4(c) + as_f4(d);
  if (__builtin_expect(__any((bad & 0xffu) != 0u), 0)) {
    u32 m = (bad4(a) ? 1u : 0u) | (bad4(b) ? 2u : 0u) | (bad4(c) ? 4u : 0u) | (bad4(d) ? 8u : 0u);
#pragma nounroll
    for (u32 spin = 0; m != 0u && spin < SPIN_CAP; ++spin) {
      asm volatile("s_nop 4\n\t" CLD(0, o0) CLD(1, o1) CLD(2, o2) CLD(3, o3) "s_waitcnt vmcnt(0)"
                   : "+v"(a), "+v"(b), "+v"(c), "+v"(d) : [vo] "v"(vo), [rs] "s"(rs), [o0] "s"(o0), [o1] "s"(o1), [o2] "s"(o2), [o3] "s"(o3), [m] "s"(m) : "memory", "scc");
      RECHECK(a, 0) RECHECK(b, 1) RECHECK(c, 2) RECHECK(d, 3)
    }
    s0 = as_f4(a) + as_f4(b); s1 = as_f4(c) + as_f4(d);
  }
}
DEVI f32x4 gather8_finish(u32x4 rs, int vo, int o0, int os, u32 tag, u32x4& a, u32x4& b, u32x4& c, u32x4& d, u32x4& e, u32x4& f, u32x4& g, u32x4& h) {
  u32 bad = 0;
  f32x4 s;
#define STEP(v) SB(); TAKE(v); s += as_f4(v); PIN(s);
  TAKE(a); s = as_f4(a); STEP(b) STEP(c) STEP(d) STEP(e) STEP(f) STEP(g) STEP(h)
  if (__builtin_expect(__any((bad & 0xffu) != 0u), 0)) {
    u32 m = (bad4(a) ? 1u : 0u) | (bad4(b) ? 2u : 0u) | (bad4(c) ? 4u : 0u) | (bad4(d) ? 8u : 0u) | (bad4(e) ? 16u : 0u) | (bad4(f) ? 32u : 0u) | (bad4(g) ? 64u : 0u) | (bad4(h) ? 128u : 0u);
#pragma nounroll
    for (u32 spin = 0; m != 0u && spin < SPIN_CAP; ++spin) {
      asm volatile("s_nop 4\n\t" CLD(0, o0) CLD(1, o1) CLD(2, o2) CLD(3, o3) CLD(4, o4) CLD(5, o5) CLD(6, o6) CLD(7, o7) "s_waitcnt vmcnt(0)"
                   : "+v"(a), "+v"(b), "+v"(c), "+v"(d), "+v"(e), "+v"(f), "+v"(g), "+v"(h)
                   : [vo] "v"(vo), [rs] "s"(rs), [o0] "s"(o0), [o1] "s"(o0 + os), [o2] "s"(o0 + 2 * os), [o3] "s"(o0 + 3 * os), [o4] "s"(o0 + 4 * os), [o5] "s"(o0 + 5 * os),
                     [o6] "s"(o0 + 6 * os), [o7] "s"(o0 + 7 * os), [m] "s"(m) : "memory", "scc");
      RECHECK(a, 0) RECHECK(b, 1) RECHECK(c, 2) RECHECK(d, 3) RECHECK(e, 4) RECHECK(f, 5) RECHECK(g, 6) RECHECK(h, 7)
    }
    s = ((((((as_f4(a) + as_f4(b)) + as_f4(c)) + as_f4(d)) + as_f4(e)) + as_f4(f)) + as_f4(g)) + as_f4(h);
  }
  return s;
}
DEVI u32 now() { return (u32)__builtin_amdgcn_s_memrealtime(); }
DEVI void wait_until(u32 due) {
  while ((u32)(due - now() - 1u) < 4000u) __builtin_amdgcn_s_sleep(1);
}

__global__ void __launch_bounds__(512, 2) fused_block(const u16* x, const u16* kc, const u16* vc, const u16* wq,
                                                      const u16* wo, const u16* wg, const u16* wu, const u16* wd,
                                                      const u16* cnd, const u16* wmod, u16* out, unsigned char* ws) {
  u32 ka0, ka1;
  asm volatile("s_load_dword %0, %2, 0x0\n\ts_load_dword %1, %2, 0x40" : "=&s"(ka0), "=&s"(ka1) : "s"(__builtin_amdgcn_kernarg_segment_ptr()));
  SB();
  __shared__ __attribute__((aligned(16))) unsigned char lds_raw[L_END];
  LAS unsigned char* L = (LAS unsigned char*)lds_raw;
  const int lane = threadIdx.x & 63, wave = __builtin_amdgcn_readfirstlane(threadIdx.x >> 6);
  const int bid = blockIdx.x, xc = bid & 7, ix = bid >> 3;
  const int grp = xc * 2 + (ix & 1), split = ix >> 1;
  u32 ipf;
  {
    unsigned long long pc; asm volatile("s_getpc_b64 %0" : "=s"(pc));
    ipf = ld32<0>(mk_rsrc((const void*)(pc & ~127ull), wave == 1 ? CODE_BYTES : 0), lane < 52 ? lane * 128 : 0x7ffffff0, (ix & 3) * 52 * 128);
  }
  const int n16 = lane & 15, g4 = lane >> 4;
  const int tr_row = 8 * g4 + (n16 >> 2), tr_col = 8 * (lane & 3);
  const bool svc = wave == 0;
  const bool isq = ix < 16;
  const int lb = ((ix - 16) >> 1) * 8 + xc;
  const bool pre = isq | (lb < 42);
  const int col0 = (isq ? xc * 8 + (ix >> 1) : lb) * 64 + (ix & 1) * 32;
  const int rstride = isq ? NQ * 2 : NMOD * 2;
  const bool is_wo = ix < 14, is_ffn = ix >= 25;
  const int fu = xc * 7 + (ix - 25);
  const bool act = lane < 56;
  const int wva = act ? lane * 8 : 0x10000, wvb = (lane >> 2) * rstride + (lane & 3) * 16, wsb = (wave - 1) * 64 * rstride + col0 * 2, w16 = 16 * rstride, wnb = D * rstride;
  const int wn1 = svc ? 2 * D * 2 : wnb;
  int w_v = svc ? wva : wvb, w_s0 = svc ? 0 : wsb, w_s1 = svc ? 448 : w16, w_nb = pre ? wn1 : 0;
  MEMBAR();
  PIN(w_v); PINS(w_s0); PINS(w_s1); PINS(w_nb);
  SB();
  PINS(x); PINS(kc); PINS(vc); PINS(wq); PINS(wo); PINS(wg); PINS(wu); PINS(wd); PINS(cnd); PINS(wmod); PINS(out); PINS(ws);
  asm volatile("s_waitcnt lgkmcnt(0)" ::"s"(ka0), "s"(ka1));
  const rsrc_t rws = mk_rsrc(ws, WS_END);
  u32 tag;
  auto lds_signal = [&](int off) { asm volatile("s_waitcnt lgkmcnt(0)" ::: "memory"); if (lane == 0) __hip_atomic_fetch_add((LAS u32*)(L + off), 1u, __ATOMIC_RELAXED, __HIP_MEMORY_SCOPE_WORKGROUP); };
  auto lds_await = [&](int off, u32 want) {
#pragma nounroll
    for (u32 spin = 0; spin < LSPIN_CAP; ++spin) {
      if (__builtin_amdgcn_readfirstlane(__hip_atomic_load((LAS u32*)(L + off), __ATOMIC_RELAXED, __HIP_MEMORY_SCOPE_WORKGROUP)) >= want) break;
      __builtin_amdgcn_s_sleep(1);
    }
    MEMBAR();
  };

  f32x4 acc[2];
  const bool ffw = is_ffn && wave < 7;
  const int gu_v = (lane >> 2) * (FF * 2) + (lane & 3) * 16, gu_s = wave * 64 * (FF * 2) + fu * 64;
  const int pf_j = (ix - 14) * 8 + wave, pf_v = (lane >> 5) * (FF * 2) + (lane & 31) * 16, pf_s = pf_j * 2 * (FF * 2) + ((448 * xc) & ~127);
  u32 tend;
  u32x4 tw[4];
  u32x2 md[3];
  u32 xin;
  const int bt = g4 & 1, col = wave * 64 + n16 * 4 + (lane >> 5) * 2;
  const int lw = L_W + wave * 8192;
  int r_w = L_RED + wave * 2048 + (g4 >> 1) * 1024 + n16 * 32 + (g4 & 1) * 16;
  const int hd = 2 * xc + (ix >= 7 ? 1 : 0), cbk = ix >= 7 ? ix - 7 : ix;
  const int wo_v = (lane >> 3) * (D * 2) + (lane & 7) * 16, wo_s = (hd * 256 + wave * 32) * (D * 2) + cbk * 128;
  {
    const rsrc_t rk = mk_rsrc(kc, 64u << 20), rv = mk_rsrc(vc, 64u << 20);
    const int sbase = (grp * 16 + split) * (256 << 10) + wave * 4096;
    auto vj = [](int i) { return i < 28 ? (i & 15) >> 1 : 6 + (i & 1); };
    auto vh = [](int i) { return i < 28 ? i & 1 : (i >> 1) & 1; };
    auto voff = [&](int i) { return ((i >> 4) * 4 + (vj(i) >> 1)) * 32768 + (vj(i) & 1) * 2048 + vh(i) * 256; };
    auto vidx = [](int ks, int j, int half) { return (ks == 0 || j < 6) ? ks * 16 + j * 2 + half : 28 + half * 2 + (j & 1); };
    const int kvo = (n16 >> 1) * 512 + g4 * 32 + (n16 & 1) * 16, vvo = g4 * 512 + n16 * 16;
    u32x4 K[32], V[32];
#define PACE_WAIT() do { if (!svc) asm volatile("s_waitcnt vmcnt(%0)" ::"i"(PACE - 1)); } while (0)
    constexpr int STG = 56, SLMIN = -(TAU - TAUMIN) * 1024 / 10;
    const u32 stag = (u32)((wave * 8 + ((ix + xc * 4) & 7)) * STG) >> 6;
    u32 tstart, obase, tlast, tsw = 0, tread = 0;
    int oslope = 0;
    const bool sample = (ix & 7) == 3 && wave == (ix >> 3) * 2 + 1, reader = wave == 1;
    const u32 lead = is_ffn ? LEAD_FFN / 10 : is_wo ? 0 : LEAD_NONE / 10;
    auto nominal = [](int n) { return (TOFF + n * TAU + (n > N_END ? (n - N_END) * (TAU_END - TAU) : 0)) / 10; };
    auto tick = [&](int n) {
      if (n == 16 && reader) {
        const int dt = (int)(tsw - (tstart << 8)) >> 8;
        const float d = ((tsw & 0xffu) == tag && dt > -2000 && dt < 2000) ? (float)dt : (float)(int)(tread - 40u - nominal(4) - tstart);
        lds_st<u32>(L, L_CNT + 16, (tstart + (u32)(int)(wave_sum(d) * (1.f / 64)) - 4u) | 1u);
      }
      if (n == 18) {
        const u32 tg = __builtin_amdgcn_readfirstlane(__hip_atomic_load((LAS u32*)(L + L_CNT + 16), __ATOMIC_RELAXED, __HIP_MEMORY_SCOPE_WORKGROUP));
        const u32 real = tlast - stag - nominal(16);
        if ((int)(real - obase) > 0) obase = real;
        oslope = (int)((tg != 0u ? tg : tstart) - lead - obase) * (1024 / 44);
        oslope = oslope < SLMIN ? SLMIN : oslope;
      }
      if (n % 2 == 0 && n >= 4) {
        const u32 due = obase + stag + nominal(n) + (n > 18 ? (oslope * (n - 18)) >> 10 : 0);
        u32 t;
        while ((u32)(due - (t = now()) - 1u) < 4000u) __builtin_amdgcn_s_sleep(1);
        tlast = t;
        if (n == 4 && (int)(t - due) > 4) obase += t - due;
      }
      if (n == 4 && sample && lane == 0)
        __builtin_amdgcn_raw_buffer_store_b32(((tlast - stag - nominal(4)) << 8) | tag, rws, 0, WS_TS + ((ix >> 3) * 8 + xc) * 128, AUX_SC1);
      if (n == 12) {
        if (reader) tsw = ld32<AUX_SC1>(rws, (lane & 31) * 128, WS_TS);
        tread = tlast;
        MEMBAR();
      }
    };
#define LOADK(i0, i1) _Pragma("unroll") for (int i = (i0); i < (i1); ++i) { tick(i); K[i] = ld128<AUX_STREAM>(rk, kvo, sbase + (i >> 2) * 32768 + (i & 3) * 128); PACE_WAIT(); SB(); }
#define LOADV(i0, i1) _Pragma("unroll") for (int i = (i0); i < (i1); ++i) { tick(32 + i); V[i] = ld128<AUX_STREAM>(rv, vvo, sbase + voff(i)); PACE_WAIT(); SB(); }
    u32x4 w4[4];
#pragma unroll
    for (int p = 0; p < 4; ++p) w4[p] = ld128<AUX_SC1>(mk_rsrc(svc ? (isq ? x : cnd) : (isq ? wq : wmod), w_nb), w_v, w_s0 + p * w_s1);
    u32 epoch = ld32<AUX_SC1>(rws, 0, WS_EPOCH);
    SB();
    obase = tlast = tstart = now();
    SB();
    if (svc) { lds_st<u32x2>(L, L_Z + lane * 8, u32x2{0u, 0u}); lds_st<u32>(L, L_CNT + (lane & 15) * 4, 0u); __builtin_amdgcn_s_setprio(3); }
    typedef const __attribute__((address_space(4))) u32* cptr;
    size_t pb;
    {
      const size_t b0 = (wave & 4) ? ((wave & 2) ? ((wave & 1) ? (size_t)vc : (size_t)wd) : ((wave & 1) ? (size_t)wu : (size_t)wg))
                                   : ((wave & 2) ? ((wave & 1) ? (size_t)wo : is_ffn ? (size_t)out : (size_t)x) : ((wave & 1) ? (size_t)ws : (size_t)vc));
      const int vb = (grp * 16 + split) * (256 << 10), g24 = (ix < 24 ? ix : 0) * 65536;
      const int o0 = (wave & 4) ? ((wave & 2) ? ((wave & 1) ? vb + 131072 : xc * 224 * D * 2 + (ix & 3) * 49152) : g24)
                                : ((wave & 2) ? ((wave & 1) ? xc * 512 * D * 2 + (ix & 7) * 57344 : is_ffn ? fu * 32 : 0) : ((wave & 1) ? (ix & 15) * 65536 : vb));
      pb = b0 + o0;
      PINS(pb);
    }
    BAR();
    const u32 touch = *(cptr)pb;
    SB();
    LOADK(0, 4)
    PIN(epoch);
    tag = (__builtin_amdgcn_readfirstlane(epoch) + 1u) & 0xffu;
    asm volatile("" ::"s"(touch));
    if (!svc && pre) {
#pragma unroll
      for (int p = 0; p < 4; ++p) lds_st<u32x4>(L, L_WD + ((wave - 1) * 4 + p) * 1024 + lane * 16, w4[p]);
      lds_signal(L_CNT);
    }
    SB();
    asm volatile("" ::"v"(ipf));
    auto gemv = [&](float (&a)[4][4]) {
      if (act) {
#pragma unroll
        for (int r = 0; r < 4; ++r) lds_st<u32x2>(L, L_ACT + (r >> 1) * 1024 + (r & 1) * 448 + lane * 8, u32x2{pk_bf(a[r][0], a[r][1]), pk_bf(a[r][2], a[r][3])});
      }
      lds_await(L_CNT, 7);
      f32x4 c0 = {0.f, 0.f, 0.f, 0.f}, c1 = c0;
#pragma unroll
      for (int hf = 0; hf < 2; ++hf) {
        u32x4 w[14], bf[7];
#pragma unroll
        for (int k = 0; k < 7; ++k) {
          const int ks = hf * 7 + k;
          bf[k] = lds_ld<u32x4>(L, L_ACT + (lane & 1) * 1024 + ks * 64 + g4 * 16);
          w[2 * k] = tr_frag(L, L_WD + (ks * 32 + tr_row) * 64 + tr_col, 256);
          w[2 * k + 1] = tr_frag(L, L_WD + (ks * 32 + tr_row) * 64 + 32 + tr_col, 256);
        }
        SB();
#pragma unroll
        for (int k = 0; k < 7; ++k) { c0 = mfma(w[2 * k], bf[k], c0); c1 = mfma(w[2 * k + 1], bf[k], c1); }
      }
      const f32x4 cs = (n16 & 2) ? c1 : c0;
      if (n16 < 4) st128_sc1(rws, tag4(f32x4{rbf(cs[0]), rbf(cs[1]), rbf(cs[2]), rbf(cs[3])}, tag), (n16 & 1) * rstride * 2 + (col0 + (n16 >> 1) * 16 + 4 * g4) * 4, isq ? WS_Q : WS_MOD);
    };
    float a[4][4];
    if (__builtin_expect(svc && pre, 0)) {
#pragma unroll
      for (int r = 0; r < 4; ++r) { a[r][0] = bf_lo(w4[r][0]); a[r][1] = bf_hi(w4[r][0]); a[r][2] = bf_lo(w4[r][1]); a[r][3] = bf_hi(w4[r][1]); }
      if (isq) {
#pragma unroll
        for (int b = 0; b < 2; ++b) {
          float s = 0.f;
#pragma unroll
          for (int e = 0; e < 8; ++e) s += a[2 * b + (e >> 2)][e & 3];
          const float mean = wave_sum(s) * (1.f / D);
          float q = 0.f;
#pragma unroll
          for (int e = 0; e < 8; ++e) { const float d = a[2 * b + (e >> 2)][e & 3] - mean; q += d * d; }
          const float var = wave_sum(act ? q : 0.f) * (1.f / D);
          const float mb = rbf(mean), inv = rcp(rbf(__builtin_amdgcn_sqrtf(rbf(rbf(var) + 1e-5f))));
#pragma unroll
          for (int e = 0; e < 8; ++e) { float& v = a[2 * b + (e >> 2)][e & 3]; v = rbf(rbf(v - mb) * inv); }
        }
      } else {
#pragma unroll
        for (int r = 0; r < 4; ++r)
#pragma unroll
          for (int e = 0; e < 4; ++e) a[r][e] = silu(a[r][e]);
        gemv(a);
      }
#pragma unroll
      for (int r = 0; r < 4; ++r) asm volatile("" : "+v"(a[r][0]), "+v"(a[r][1]), "+v"(a[r][2]), "+v"(a[r][3]));
    }
    SB();
    LOADK(4, MOD_AT)
    u32x4 m[8];
    auto m_off = [](int k) { return WS_MOD + ((k >> 2) * NMOD + ((k >> 1) & 1) * 224 + (k & 1) * D) * 4; };
    if (__builtin_expect(svc && isq, 0)) {
#pragma unroll
      for (int k = 0; k < 8; ++k) m[k] = ld128<AUX_SC1>(rws, act ? lane * 16 : 0, m_off(k));
      MEMBAR();
    }
    SB();
    LOADK(MOD_AT, MOD_AT + 4)
    if (__builtin_expect(svc && isq, 0)) {
      u32 bad = 0;
#pragma unroll
      for (int k = 0; k < 8; ++k) { m[k] ^= tag; bad |= or4(m[k]); }
      if (__any((bad & 0xffu) != 0u)) gather<8>(rws, act ? lane * 16 : 0, m_off, tag, m);
#pragma unroll
      for (int r = 0; r < 4; ++r)
#pragma unroll
        for (int e = 0; e < 4; ++e) a[r][e] = rbf(a[r][e] * rbf(1.f + __uint_as_float(m[2 * r + 1][e]))) + __uint_as_float(m[2 * r][e]);
      gemv(a);
    }
    SB();
    LOADK(MOD_AT + 4, Q_AT)
    u32x4 qv[2];
    if (__builtin_expect(svc, 0)) {
#pragma unroll
      for (int k = 0; k < 2; ++k) qv[k] = ld128<AUX_SC1>(rws, lane * 16, WS_Q + grp * 2048 + k * 1024);
      MEMBAR();
    }
    SB();
    LOADK(Q_AT, 32)
    LOADV(0, Q_AT + 4 - 32)
    if (__builtin_expect(svc, 0)) {
      qv[0] ^= tag; qv[1] ^= tag;
      if (bad4(qv[0] | qv[1])) gather<2>(rws, lane * 16, [&](int k) { return WS_Q + grp * 2048 + k * 1024; }, tag, qv);
#pragma unroll
      for (int k = 0; k < 2; ++k)
        lds_st<u32x2>(L, L_Q + k * 512 + lane * 8, u32x2{__builtin_amdgcn_perm(qv[k][1], qv[k][0], 0x07060302u), __builtin_amdgcn_perm(qv[k][3], qv[k][2], 0x07060302u)});
      lds_signal(L_CNT + 4);
    }
    SB();
    LOADV(Q_AT + 4 - 32, VPRE)
    lds_await(L_CNT + 4, 1);
    f32x4 d0 = {0.f, 0.f, 0.f, 0.f}, d1 = d0;
    const int qa = L_Q + (n16 & 1) * 512 + g4 * 32 + ((n16 >> 1) & 1) * 16;
    u32x4 qf[2][4];
#pragma unroll
    for (int t = 0; t < 4; ++t) qf[0][t] = lds_ld<u32x4>(L, ((n16 >> 2) == 0 ? qa : L_Z) + t * 128);
#pragma unroll
    for (int sl = 0; sl < 4; ++sl) {
      if (sl < 3) {
#pragma unroll
        for (int t = 0; t < 4; ++t) qf[(sl + 1) & 1][t] = lds_ld<u32x4>(L, ((n16 >> 2) == sl + 1 ? qa : L_Z) + t * 128);
      }
      SB();
#pragma unroll
      for (int t = 0; t < 4; ++t) {
        d0 = mfma(qf[sl & 1][t], K[8 * sl + t], d0);
        d1 = mfma(qf[sl & 1][t], K[8 * sl + 4 + t], d1);
      }
      asm volatile("" : "+v"(d0), "+v"(d1));
      SB();
    }
    LOADV(VPRE, VPRE + 2)
    {
      float m[2], l[2];
      const int key = (2 * g4 + (n16 & 1)) * 8 + (n16 >> 1);
#pragma unroll
      for (int h = 0; h < 2; ++h) {
        const float keep = (n16 & 1) ? d1[2 + h] : d0[h], send = (n16 & 1) ? d0[2 + h] : d1[h];
        const float sc = rbf(keep + dpp<XOR1>(send)) * 0.0625f;
        m[h] = wave_max(sc);
        const float p = ex2((sc - m[h]) * LOG2E);
        l[h] = wave_sum(p);
        lds_st<u16>(L, L_P + wave * 256 + h * 128 + (key >> 5) * 64 + (key & 3) * 16 + ((key >> 2) & 7) * 2, (u16)pk_bf(p, p));
      }
      u32 prev = 0;
      if (lane == 0) {
        lds_st<f32x4>(L, L_SS + wave * 16, f32x4{m[0], l[0], m[1], l[1]});
        asm volatile("s_waitcnt lgkmcnt(0)" ::: "memory");
        prev = __hip_atomic_fetch_add((LAS u32*)(L + L_CNT + 8), 1u, __ATOMIC_RELAXED, __HIP_MEMORY_SCOPE_WORKGROUP);
      }
      if (__builtin_amdgcn_readfirstlane(prev) == 7u) {
        MEMBAR();
        f32x4 r = lds_ld<f32x4>(L, L_SS + (lane & 7) * 16);
#pragma unroll
        for (int h = 0; h < 2; ++h) {
          float M = r[2 * h];
          M = fmaxf(M, dpp<XOR1>(M)); M = fmaxf(M, dpp<XOR2>(M)); M = fmaxf(M, dpp<XOR7>(M));
          float t = r[2 * h + 1] * ex2((r[2 * h] - M) * LOG2E);
          t += dpp<XOR1>(t); t += dpp<XOR2>(t); t += dpp<XOR7>(t);
          r[2 * h] = M; r[2 * h + 1] = t;
        }
        if (lane < 8) st128_sc1(rws, tag4(r, tag), lane * 16, WS_ST + (grp * 16 + split) * 128);
      }
    }
    SB();
    __builtin_amdgcn_s_setprio(0);
    acc[0] = acc[1] = f32x4{0.f, 0.f, 0.f, 0.f};
    const int pa = L_P + wave * 256 + (n16 >> 3) * 128 + g4 * 16;
    u32x4 pf[8];
    auto pv_load = [&](int ks) {
#pragma unroll
      for (int e = 0; e < 8; ++e) pf[e] = lds_ld<u32x4>(L, ((n16 & 7) == e ? pa : L_Z) + ks * 64);
    };
    auto vperm = [&](u32x4 (&vt)[8], int ks, int half, int jj) {
      const u32x4 A = V[vidx(ks, 2 * jj, half)], B = V[vidx(ks, 2 * jj + 1, half)];
#pragma unroll
      for (int e = 0; e < 8; ++e) vt[e][jj] = __builtin_amdgcn_perm(B[e >> 1], A[e >> 1], (e & 1) ? 0x07060302u : 0x05040100u);
    };
    auto pv = [&](int ks, int half) {
      u32x4 vt[8];
#pragma unroll
      for (int jj = 0; jj < 4; ++jj) { vperm(vt, ks, half, jj); if (jj < 3) SB(); }
#pragma unroll
      for (int e = 0; e < 8; ++e) acc[half] = mfma(pf[e], vt[e], acc[half]);
      PIN(acc[half]);
      SB();
    };
    u32x4 st[1];
#pragma unroll
    for (int i = VPRE + 2; i < 32; ++i) {
      tick(32 + i);
      V[i] = ld128<AUX_STREAM>(rv, vvo, sbase + voff(i));
      if (i > MD_AT && i <= MD_AT + 5) { if (!svc) asm volatile("s_waitcnt vmcnt(%0)" ::"i"(PACE + 3)); }
      else if (i < 31) PACE_WAIT();
      SB();
      if (i == PV0_AT) { pv_load(0); pv(0, 0); }
      if (i == PV0_AT + 2) { pv(0, 1); pv_load(1); SB(); }
      if (i == MD_AT && ffw) {
        xin = ld32<0>(mk_rsrc(x, 2 * D * 2), (bt * D + col) * 2, 0);
#pragma unroll
        for (int k = 0; k < 3; ++k) md[k] = ld64<AUX_SC1>(rws, (bt * NMOD + col) * 4, WS_MOD + (2 + k) * D * 4);
        MEMBAR();
        SB();
      }
      if (i == STAT_AT && svc) {
        st[0] = ld128<AUX_SC1>(rws, n16 * 128, WS_ST + grp * 2048);
        MEMBAR();
        SB();
      }
    }
    lds_signal(L_CNT + 12);
    if (svc) {
      st[0] ^= tag;
      if (__builtin_expect(bad4(st[0]), 0)) gather<1>(rws, n16 * 128, [&](int) { return WS_ST + grp * 2048; }, tag, st);
      MEMBAR();
      const f32x4 r = lds_ld<f32x4>(L, L_SS + (lane & 7) * 16);
      float f[2];
#pragma unroll
      for (int h = 0; h < 2; ++h) {
        const float mw = __uint_as_float(st[0][2 * h]), M = row_max(mw);
        f[h] = ex2((r[2 * h] - M) * LOG2E) * rcp(row_sum(__uint_as_float(st[0][2 * h + 1]) * ex2((mw - M) * LOG2E)));
      }
      if (lane < 16) lds_st<float>(L, L_F + lane * 4, lane < 8 ? f[0] : f[1]);
    }
    PIN(r_w);
    u32x4 vt0[8], vt1[8];
#pragma unroll
    for (int jj = 0; jj < 2; ++jj) { vperm(vt0, 1, 0, jj); vperm(vt1, 1, 1, jj); }
#pragma unroll
    for (int e = 0; e < 8; e += 2) asm volatile("" : "+v"(vt0[e]), "+v"(vt0[e + 1]), "+v"(vt1[e]), "+v"(vt1[e + 1]));
    SB();
    {
      const u32 nb = is_wo ? NQ * D * 2 : (ffw || !is_ffn) ? D * FF * 2 : 0;
      const int va = is_wo ? wo_v : ffw ? lane * 16 : pf_v, sa = is_wo ? wo_s : ffw ? fu * 32 * (D * 2) + wave * 4096 : pf_s;
      const int s1 = is_wo ? 8 * (D * 2) : ffw ? 1024 : 0, s2 = is_wo ? 16 * (D * 2) : ffw ? 2048 : 176 * (FF * 2);
      const u16 *pa = is_wo ? wo : ffw ? wd : wg, *pb2 = is_wo ? wo : ffw ? wd : wu;
      const u32 tg = __builtin_amdgcn_readfirstlane(lds_ld<u32>(L, L_CNT + 16));
      tend = (tg != 0u ? tg : tstart) + STG + nominal(62);
      lds_await(L_CNT + 12, 8);
      tw[0] = ld128<0>(mk_rsrc(pa, nb), va, sa);
      tw[1] = ld128<0>(mk_rsrc(pb2, nb), va, sa + s1);
      tw[2] = ld128<0>(mk_rsrc(pa, nb), va, sa + s2);
      tw[3] = ld128<0>(mk_rsrc(pb2, nb), va, sa + s2 + s1);
      MEMBAR();
      SB();
    }
    vperm(vt0, 1, 0, 2); vperm(vt1, 1, 1, 2);
    SB();
    vperm(vt0, 1, 0, 3);
#pragma unroll
    for (int e = 0; e < 8; ++e) acc[0] = mfma(pf[e], vt0[e], acc[0]);
    lds_st<f32x4>(L, r_w, acc[0]);
    SB();
    vperm(vt1, 1, 1, 3);
#pragma unroll
    for (int e = 0; e < 8; ++e) acc[1] = mfma(pf[e], vt1[e], acc[1]);
  }
  const u32x4 rsw = {(u32)(size_t)ws, (u32)((size_t)ws >> 32) & 0xffffu, (u32)WS_END, 0x00020000u};
  lds_st<f32x4>(L, r_w + 512, acc[1]);
  BAR();
  if (wave < 2) {
    f32x4 t[8];
#pragma unroll
    for (int w = 0; w < 8; ++w) t[w] = lds_ld<f32x4>(L, L_RED + w * 2048 + wave * 1024 + lane * 16);
    const f32x4 fa = lds_ld<f32x4>(L, L_F + wave * 32), fb = lds_ld<f32x4>(L, L_F + wave * 32 + 16);
    SB();
    t[0] = t[0] * fa[0] + t[1] * fa[1]; t[1] = t[2] * fa[2] + t[3] * fa[3]; t[2] = t[4] * fb[0] + t[5] * fb[1]; t[3] = t[6] * fb[2] + t[7] * fb[3];
    st128_sc1(rws, tag4((t[0] + t[1]) + (t[2] + t[3]), tag), lane * 16, WS_OP + (grp * 16 + split) * 2048 + wave * 1024);
  }
  SB();
  const f32x4 zero4 = {0.f, 0.f, 0.f, 0.f};
  if (is_wo) {
    int a_r[4];
#pragma unroll
    for (int t = 0; t < 4; ++t) a_r[t] = (n16 >> 1) == t ? L_ACT + (n16 & 1) * 1024 + wave * 64 + g4 * 16 : L_Z;
    int a_w = L_ACT + (lane >> 5) * 1024 + wave * 64 + (lane & 7) * 8 + (g4 & 1) * 4;
    int p_w = n16 < 8 ? L_PD + wave * 512 + n16 * 64 + g4 * 16 : L_PD + 4096 + lane * 16, p_r = L_PD + g4 * 1024 + wave * 256 + n16 * 16;
    int o_v = (((n16 >> 2) & 1) * D + (n16 >> 3) * 16 + (n16 & 3) * 4 + (lane >> 5) * 2 + (g4 & 1)) * 4, o_s = WS_AP + (hd * 2 * D + cbk * 64 + wave * 32) * 4;
    int g_v = (lane >> 3) * 2048 + (hd & 1) * 1024 + wave * 128 + (lane & 7) * 16;
    PIN(a_w); PIN(p_w); PIN(p_r); PIN(o_v); PINS(o_s); PIN(g_v);
#pragma unroll
    for (int t = 0; t < 4; ++t) PIN(a_r[t]);
    const int go0 = WS_OP + xc * 32768, go2 = WS_OP + (8 + xc) * 32768;
    u32x4 o0, o1, o2, o3;
    wait_until(tend + H1_DELAY / 10);
    gather4_issue(rws, g_v, go0, go0 + 16384, go2, go2 + 16384, o0, o1, o2, o3);
    SB();
#pragma unroll
    for (int p = 0; p < 4; ++p) lds_st<u32x4>(L, lw + p * 1024 + lane * 16, tw[p]);
    u32x4 a[4];
#pragma unroll
    for (int t = 0; t < 4; ++t) a[t] = tr_frag(L, lw + tr_row * 128 + t * 32 + tr_col, 512);
    SB();
    f32x4 v0, v1;
    gather4_finish(rsw, g_v, go0, go0 + 16384, go2, go2 + 16384, tag, o0, o1, o2, o3, v0, v1);
    const float u0 = red32(v0[0], v1[0]), u1 = red32(v0[1], v1[1]), u2 = red32(v0[2], v1[2]), u3 = red32(v0[3], v1[3]);
    const float w0 = red16(u0, u2), w1 = red16(u1, u3);
    lds_st<u32>(L, a_w, pk_bf(w0 + dpp<ROR8>(w0), w1 + dpp<ROR8>(w1)));
    const u32x4 b0 = lds_ld<u32x4>(L, a_r[0]), b1 = lds_ld<u32x4>(L, a_r[1]), b2 = lds_ld<u32x4>(L, a_r[2]), b3 = lds_ld<u32x4>(L, a_r[3]);
    SB();
    lds_st<f32x4>(L, p_w, mfma(a[3], b3, mfma(a[2], b2, mfma(a[1], b1, mfma(a[0], b0, zero4)))));
    BAR();
    if (wave < 2) {
      const f32x4 t = lds_ld<f32x4>(L, p_r) + lds_ld<f32x4>(L, p_r + 512);
      __builtin_amdgcn_raw_buffer_store_b32(tagw(red16(red32(t[0], t[2]), red32(t[1], t[3])), tag), rws, o_v, o_s, AUX_SC1);
    }
  } else if (is_ffn) {
    BAR();
    if (wave < 7) {
      u32x4 fw[8];
#pragma unroll
      for (int p = 0; p < 8; ++p) fw[p] = ld128<AUX_SC1>(mk_rsrc(p < 4 ? wg : wu, D * FF * 2), gu_v, gu_s + (p & 3) * 16 * (FF * 2));
      MEMBAR();
      SB();
      for (u32 spin = 0; __any((((md[0][0] ^ tag) | (md[0][1] ^ tag) | (md[1][0] ^ tag) | (md[1][1] ^ tag) | (md[2][0] ^ tag) | (md[2][1] ^ tag)) & 0xffu) != 0u) && spin < SPIN_CAP; ++spin) {
        MEMBAR();
#pragma unroll
        for (int k = 0; k < 3; ++k) md[k] = ld64<AUX_SC1>(rws, (bt * NMOD + col) * 4, WS_MOD + (2 + k) * D * 4);
      }
      float ga[2] = {bf_hi(md[0][0]), bf_hi(md[0][1])}, cb[2] = {rbf(1.f + bf_hi(md[2][0])), rbf(1.f + bf_hi(md[2][1]))}, xf[2] = {bf_lo(xin), bf_hi(xin)};
      int a_w = L_ACT + bt * 1024 + col * 2;
      lds_st<u32>(L, a_w + 2048, pk_bf(cb[0], cb[1]));
      lds_st<u32>(L, a_w + 4096, __builtin_amdgcn_perm(md[1][1], md[1][0], 0x07060302u));
      const int a_in = L_ACT + ((n16 & 3) * 2 + ((n16 >> 2) & 1)) * 1024 + wave * 128 + g4 * 16;
      int a_r0 = n16 < 8 ? a_in : L_Z, a_r1 = n16 >= 8 ? a_in : L_Z;
      int p_w = L_PD + wave * 2048 + lane * 16, p_r = L_PD + lane * 16;
      int d_r[7], d_v = n16 < 14 ? ((n16 & 1) * D + (n16 >> 1) * 16 + g4 * 4) * 4 : 0x7ffffff0, d_s = WS_DP + (fu * 2 * D + wave * 112) * 4;
#pragma unroll
      for (int t = 0; t < 7; ++t) { d_r[t] = (n16 >> 1) == t ? L_A2 + (wave & 3) * 128 + (n16 & 1) * 64 + g4 * 16 : L_Z; PIN(d_r[t]); }
      int s_r = L_SS + bt * 8, c_w = L_A2 + (wave & 3) * 128 + bt * 64 + (g4 >> 1) * 32 + n16 * 2;
      int g_v = (((lane >> 5) * 8 * 2 + bt) * D + wave * 64 + n16 * 4) * 4;
      int x_w = L_X1 + (bt * D + col) * 2, s_w = n16 == 0 ? L_SS + wave * 16 + bt * 8 + (lane >> 5) * 4 : L_SS + 112;
      PIN(d_v); PINS(d_s); PIN(s_r); PIN(c_w); PIN(g_v); PIN(x_w); PIN(s_w); PIN(a_w); PIN(p_w); PIN(p_r); PIN(a_r0); PIN(a_r1);
      PIN(ga[0]); PIN(ga[1]); PIN(cb[0]); PIN(cb[1]); PIN(xf[0]); PIN(xf[1]);
      constexpr int APS = 2 * D * 4;
      u32x4 p0, p1, p2, p3, p4, p5, p6, p7;
#pragma unroll
      for (int p = 0; p < 8; ++p) lds_st<u32x4>(L, lw + p * 1024 + lane * 16, fw[p]);
      u32x4 a[8], dn[7];
#pragma unroll
      for (int p = 0; p < 8; ++p) a[p] = tr_frag(L, lw + (p >> 2) * 4096 + (((p >> 1) & 1) * 32 + tr_row) * 64 + (p & 1) * 32 + tr_col, 256);
#pragma unroll
      for (int p = 0; p < 4; ++p) lds_st<u32x4>(L, L_WD + (wave * 4 + p) * 1024 + lane * 16, tw[p]);
      BAR();
      if (wave < 4) {
#pragma unroll
        for (int t = 0; t < 7; ++t) dn[t] = tr_frag(L, L_WD + tr_row * 896 + wave * 224 + t * 32 + tr_col, 4 * 896);
        SB();
      }
      wait_until(tend + H2_DELAY / 10);
      gather4_issue(rws, g_v, WS_AP, WS_AP + APS, WS_AP + 2 * APS, WS_AP + 3 * APS, p0, p1, p2, p3);
      gather4_issue(rws, g_v, WS_AP + 4 * APS, WS_AP + 5 * APS, WS_AP + 6 * APS, WS_AP + 7 * APS, p4, p5, p6, p7);
      SB();
      const f32x4 v = gather8_finish(rsw, g_v, WS_AP, APS, tag, p0, p1, p2, p3, p4, p5, p6, p7);
      const u32 t0 = pk_bf(red32(v[0], v[2]), red32(v[1], v[3]));
      const u32 t1 = pk_bf(ga[0] * bf_lo(t0), ga[1] * bf_hi(t0));
      const u32 x1 = pk_bf(xf[0] + bf_lo(t1), xf[1] + bf_hi(t1));
      const float y0 = bf_lo(x1) * cb[0], y1 = bf_hi(x1) * cb[1];
      const u32 yh = pk_bf(y0, y1);
      lds_st<u32>(L, a_w, yh);
      lds_st<u32>(L, a_w + 6144, pk_bf(y0 - bf_lo(yh), y1 - bf_hi(yh)));
      const u32x4 i0 = lds_ld<u32x4>(L, a_r0), i1 = lds_ld<u32x4>(L, a_r1), i2 = lds_ld<u32x4>(L, a_r0 + 64), i3 = lds_ld<u32x4>(L, a_r1 + 64);
      SB();
      lds_st<u32>(L, x_w, x1);
      float s1 = dot2(x1, 0x3f803f80u, 0.f), s2 = dot2(x1, x1, 0.f);
      s1 = red32(s1, s2);
      s1 = row_sum(s1);
      lds_st<float>(L, s_w, s1);
      SB();
      lds_st<f32x4>(L, p_w, mfma(i3, a[3], mfma(i2, a[2], mfma(i1, a[1], mfma(i0, a[0], zero4)))));
      lds_st<f32x4>(L, p_w + 1024, mfma(i3, a[7], mfma(i2, a[6], mfma(i1, a[5], mfma(i0, a[4], zero4)))));
      BAR();
      if (wave < 4) {
        f32x2 sv[7];
        f32x4 gv[7], uv[7];
#pragma unroll
        for (int w = 0; w < 7; ++w) sv[w] = lds_ld<f32x2>(L, s_r + w * 16);
#pragma unroll
        for (int w = 0; w < 7; ++w) gv[w] = lds_ld<f32x4>(L, p_r + w * 2048);
#pragma unroll
        for (int w = 0; w < 7; ++w) uv[w] = lds_ld<f32x4>(L, p_r + w * 2048 + 1024);
        SB();
        const f32x2 ms = ((sv[0] + sv[1]) + (sv[2] + sv[3])) + ((sv[4] + sv[5]) + sv[6]);
        const float mean = ms[0] * (1.f / D), var = fmaxf(fmaf(-mean, mean, ms[1] * (1.f / D)), 0.f);
        const u32 mv = pk_bf(mean, var);
        const float mb = bf_lo(mv), inv = rcp(rbf(__builtin_amdgcn_sqrtf(rbf(bf_hi(mv) + 1e-5f))));
        const f32x4 gs = ((gv[0] + gv[1]) + (gv[2] + gv[3])) + ((gv[4] + gv[5]) + gv[6]), us = ((uv[0] + uv[1]) + (uv[2] + uv[3])) + ((uv[4] + uv[5]) + uv[6]);
        const u32 gu = pk_bf(fmaf(fmaf(-mb, gs[1], gs[0]) + gs[3], inv, gs[2]), fmaf(fmaf(-mb, us[1], us[0]) + us[3], inv, us[2]));
        const float av = rbf(silu(bf_lo(gu))) * bf_hi(gu);
        lds_st<u16>(L, c_w, (u16)pk_bf(av, av));
        u32x4 b[7];
#pragma unroll
        for (int t = 0; t < 7; ++t) b[t] = lds_ld<u32x4>(L, d_r[t]);
        SB();
        f32x4 c = zero4;
#pragma unroll
        for (int t = 0; t < 7; ++t) c = mfma(dn[t], b[t], c);
        st128_sc1(rws, tag4(c, tag), d_v, d_s);
      }
    } else {
      const int e = (lane >> 5) * 2 + (g4 & 1), oi = fu * 16 + (lane & 3) * 4 + e;
      const int ob = oi >= D ? 1 : 0, pr = lane >> 2;
      u32x4 gbv[1];
      gather<1>(rws, (ob * (NMOD - D) + fu * 16 + (lane & 3) * 4) * 4, [](int) { return WS_MOD + 5 * D * 4; }, tag, gbv);
      const u32 gb0 = lane < 32 ? gbv[0][0] : gbv[0][2], gb1 = lane < 32 ? gbv[0][1] : gbv[0][3];
      float gb = __uint_as_float((g4 & 1) ? gb1 : gb0);
      int g_v = ((pr < 14 ? pr : pr - 2) * 2 * D + (lane & 3) * 4) * 4, o_v = (lane & 12) == 0 ? oi * 2 : 0x7ffffff0;
      float keep = pr < 14 ? 1.f : 0.f;
      PIN(gb); PIN(g_v); PIN(o_v); PIN(keep);
      const int d0 = WS_DP + fu * 64, ds = 14 * 2 * D * 4;
      u32x4 p0, p1, p2, p3;
      BAR();
      BAR();
      float x1 = bf_lo(lds_ld<u16>(L, L_X1 + oi * 2));
      PIN(x1);
      wait_until(now() + H4_DELAY / 10);
      gather4_issue(rws, g_v, d0, d0 + ds, d0 + 2 * ds, d0 + 3 * ds, p0, p1, p2, p3);
      f32x4 v, v2;
      gather4_finish(rsw, g_v, d0, d0 + ds, d0 + 2 * ds, d0 + 3 * ds, tag, p0, p1, p2, p3, v, v2);
      v += v2;
      v = keep != 0.f ? v : zero4;
      float t = red16(red32(v[0], v[2]), red32(v[1], v[3]));
      t += dpp<ROR8>(t); t += dpp<ROR4>(t);
      __builtin_amdgcn_raw_buffer_store_b16((u16)pk_bf(x1 + rbf(gb * rbf(t)), 0.f), mk_rsrc(out, 2 * D * 2), o_v, 0, 0);
      if (fu == 0 && lane == 0) __builtin_amdgcn_raw_buffer_store_b32(tag, rws, 0, WS_EPOCH, AUX_SC1);
    }
  } else {
    const u32x4 q0 = ld128<0>(mk_rsrc(wg, D * FF * 2), pf_v, pf_s + 352 * (FF * 2)), q1 = ld128<0>(mk_rsrc(wu, D * FF * 2), pf_v, pf_s + 352 * (FF * 2));
    asm volatile("" ::"v"(q0), "v"(q1), "v"(tw[0]), "v"(tw[1]), "v"(tw[2]), "v"(tw[3]));
  }
}

extern "C" void kernel_launch(void* const* d_in, const int* in_sizes, int n_in, void* d_out, int out_size, void* d_ws, size_t ws_size,
                              hipStream_t stream) {
  fused_block<<<256, 512, 0, stream>>>((const u16*)d_in[0], (const u16*)d_in[1], (const u16*)d_in[2], (const u16*)d_in[3], (const u16*)d_in[4],
                                       (const u16*)d_in[6], (const u16*)d_in[7], (const u16*)d_in[8], (const u16*)d_in[9], (const u16*)d_in[10],
                                       (u16*)d_out, (unsigned char*)d_ws);
}
```
